```python
import torch

def _softmax(x, axis=-1):
    return torch.softmax(x, dim=axis)

def _gelu(x):
    return torch.nn.functional.gelu(x, approximate='tanh')

def _silu(x):
    return torch.nn.functional.silu(x)

def _sigmoid(x):
    return torch.sigmoid(x)

def _layernorm(x, axis=-1, eps=1e-05):
    m = x.mean(dim=axis, keepdim=True)
    v = x.var(dim=axis, keepdim=True, unbiased=False)
    return (x - m) / torch.sqrt(v + eps)

def _scatter_add(tgt, idx, src, axis=0):
    return tgt.clone().index_add_(axis, idx, src)

def _segment_sum(src, seg, n):
    out = torch.zeros((n,) + src.shape[1:], dtype=src.dtype)
    return out.index_add_(0, seg, src)

def _pad(x, pw):
    flat = tuple((v for pair in reversed(pw) for v in pair))
    return torch.nn.functional.pad(x, flat)
kernel_name = 'standalone__tree_speculative_verify_tree_attention_over_candidate_token_tree__sampling'

def setup_inputs():

    _w = lambda a: (lambda t: t.to(torch.bfloat16) if t.is_floating_point() else t)(torch.as_tensor(a))
    B, Hq, Hkv, T, C, D = (16, 32, 8, 8, 2048, 128)
    tree_mask = torch.zeros((B, T, T), dtype=float)
    for b in range(B):
        parent = torch.zeros(T, dtype=torch.int64)
        for i in range(1, T):
            parent[i] = int(torch.randint(0, i, (1,)).item())
        for i in range(T):
            j = i
            tree_mask[b, i, j] = 1.0
            while j > 0:
                j = parent[j]
                tree_mask[b, i, j] = 1.0
    return {'q': _w((lambda *s: torch.randn(*s, dtype=torch.bfloat16) * 0.8)(B, Hq, T, D)), 'k_cache': _w((lambda *s: torch.randn(*s, dtype=torch.bfloat16) * 0.8)(B, Hkv, C, D)), 'v_cache': _w((lambda *s: torch.randn(*s, dtype=torch.bfloat16) * 0.05)(B, Hkv, C, D)), 'k_tree': _w((lambda *s: torch.randn(*s, dtype=torch.bfloat16) * 0.8)(B, Hkv, T, D)), 'v_tree': _w((lambda *s: torch.randn(*s, dtype=torch.bfloat16) * 0.05)(B, Hkv, T, D)), 'tree_mask': _w(tree_mask)}

def reference(q, k_cache, v_cache, k_tree, v_tree, tree_mask):
    B, Hq, T, D = q.shape
    Hkv, C = (k_cache.shape[1], k_cache.shape[2])
    rep = Hq // Hkv
    kc = (lambda x, n, axis: torch.repeat_interleave(x, n, dim=axis))(k_cache, rep, axis=1)
    vc = (lambda x, n, axis: torch.repeat_interleave(x, n, dim=axis))(v_cache, rep, axis=1)
    kt = (lambda x, n, axis: torch.repeat_interleave(x, n, dim=axis))(k_tree, rep, axis=1)
    vt = (lambda x, n, axis: torch.repeat_interleave(x, n, dim=axis))(v_tree, rep, axis=1)
    k = torch.concatenate([kc, kt], axis=2)
    v = torch.concatenate([vc, vt], axis=2)
    scale = D ** (-0.5)
    scores = torch.einsum('bhqd,bhkd->bhqk', q, k) * scale
    ctx_mask = torch.ones((B, 1, T, C), dtype=scores.dtype)
    tm = tree_mask[:, None, :, :].clone()
    full_mask = torch.concatenate([ctx_mask, tm], axis=-1)
    scores = scores + (1.0 - full_mask) * -1000000000.0
    p = _softmax(scores, axis=-1)
    return torch.einsum('bhqk,bhkd->bhqd', p, v)
```

```cpp
#include <hip/hip_runtime.h>
#include <hip/hip_bf16.h>
#pragma clang diagnostic ignored "-Wint-to-pointer-cast"
#pragma clang diagnostic ignored "-Winline-asm"


typedef unsigned u32x4 __attribute__((ext_vector_type(4)));
typedef unsigned u32x2 __attribute__((ext_vector_type(2)));
typedef float f32x4 __attribute__((ext_vector_type(4)));
typedef float f32x2 __attribute__((ext_vector_type(2)));
typedef short s16x4 __attribute__((ext_vector_type(4)));
typedef __bf16 bf16x8 __attribute__((ext_vector_type(8)));
typedef __bf16 bf16x2 __attribute__((ext_vector_type(2)));
#define LDSP(T) __attribute__((address_space(3))) T*

constexpr int NT = 32, NSLOT = 6;
constexpr int W = 32, AH = 2;
constexpr float SC2 = 0.088388347648318440f * 1.4426950408889634f;
constexpr float MSKRAW = -1.0e9f / 0.088388347648318440f;
constexpr unsigned SLOT = 24576, RING = NSLOT * SLOT, L_Q = RING, L_KT = L_Q + 8192, L_VT = L_KT + 2048, L_MK = L_VT + 1024,
                   F_LAND = L_KT + 4096, F_CONS = F_LAND + 256, L_ML = F_CONS + 1024, L_OUT = L_ML + 2048, LDS_BYTES = L_OUT + 16;
constexpr unsigned L_TAIL = (NT % NSLOT) * SLOT;
constexpr int LAG = (W - 1) / 4, NGRP = 195;
constexpr int kpos(int i) { return i == 0 ? 0 : i < AH ? 3 + 4 * i : 3 + 4 * AH + 6 * (i - AH); }

__device__ __forceinline__ f32x4 mfma16(u32x4 a, u32x4 b, f32x4 c) {
  return __builtin_amdgcn_mfma_f32_16x16x32_bf16(__builtin_bit_cast(bf16x8, a), __builtin_bit_cast(bf16x8, b), c, 0, 0, 0);
}
__device__ __forceinline__ unsigned pk2(float lo, float hi) { bf16x2 v = {(__bf16)lo, (__bf16)hi}; return __builtin_bit_cast(unsigned, v); }
__device__ __forceinline__ float mx2(float a, float b) { return __builtin_elementwise_maximum(a, b); }
__device__ __forceinline__ float xg_max(float x) {
  auto s = __builtin_amdgcn_permlane16_swap(__float_as_uint(x), __float_as_uint(x), false, false);
  x = mx2(__uint_as_float(s[0]), __uint_as_float(s[1]));
  auto t = __builtin_amdgcn_permlane32_swap(__float_as_uint(x), __float_as_uint(x), false, false);
  return mx2(__uint_as_float(t[0]), __uint_as_float(t[1]));
}
__device__ __forceinline__ u32x4 tr_read2(unsigned ad) {
  const u32x2 x = __builtin_bit_cast(u32x2, __builtin_amdgcn_ds_read_tr16_b64_v4i16((LDSP(s16x4))(ad)));
  const u32x2 y = __builtin_bit_cast(u32x2, __builtin_amdgcn_ds_read_tr16_b64_v4i16((LDSP(s16x4))(ad + 2048)));
  return u32x4{x[0], x[1], y[0], y[1]};
}
__device__ __forceinline__ void land_wait(int need, int& seen) {
  while (seen < need) {
    seen = __builtin_amdgcn_readfirstlane(*(volatile LDSP(int))F_LAND);
  }
  asm volatile("" ::: "memory");
}

#define LD4(V0, V1, V2, V3, SRD, SOFF) \
  "buffer_load_dwordx4 " V0 ", " SRD ", " SOFF " offen nt lds\n\t" \
  "buffer_load_dwordx4 " V1 ", " SRD ", " SOFF " offen offset:1024 nt lds\n\t" \
  "buffer_load_dwordx4 " V2 ", " SRD ", " SOFF " offen offset:2048 nt lds\n\t" \
  "buffer_load_dwordx4 " V3 ", " SRD ", " SOFF " offen offset:3072 nt lds\n\t"
#define PUB "ds_write_b32 v10, v9\n\tv_add_u32 v9, 1, v9\n\t"
#define KGRP0 LD4("v1", "v2", "v3", "v4", "s[24:27]", "s37") "s_add_u32 m0, m0, 0x1000\n\ts_add_u32 s37, s37, 0x1000\n\t"
#define KGRP "s_waitcnt vmcnt(%c[wn])\n\t" KGRP0 PUB
#define VGRP "s_waitcnt vmcnt(%c[wn])\n\t" LD4("v5", "v6", "v7", "v8", "s[28:31]", "s38") "s_add_u32 m0, m0, 0x1000\n\ts_add_u32 s38, s38, 0x2000\n\t" PUB
#define DRAIN(N) "s_waitcnt vmcnt(" #N ")\n\t" PUB

__global__ void __launch_bounds__(320)
tree_attn(const char* kc, const char* vc, const char* q, char* ws, const char* kt, const char* vt, const char* mask, char* out) {
  __shared__ __attribute__((aligned(16))) char lds[LDS_BYTES];
  asm volatile(
      "v_cmp_gt_u32 vcc, 64, %[tid]\n\t"
      "s_cbranch_vccz .Lcompute%=\n\t"
      "s_load_dwordx16 s[8:23], %[ka], 0x0\n\t"
      "s_getpc_b64 s[4:5]\n\t"
      ".Lcode0:\n\t"
      "v_lshlrev_b32 v12, 6, %[tid]\n\t"
      "v_add_u32 v13, 0x1000, v12\n\t"
      "v_min_u32 v12, .Lcode1-.Lcode0, v12\n\t"
      "v_min_u32 v13, .Lcode1-.Lcode0, v13\n\t"
      "global_load_dword v16, v12, s[4:5]\n\t"
      "global_load_dword v16, v13, s[4:5]\n\t"
      "s_setprio 3\n\t"
      "s_and_b32 s4, %[bid], 7\n\t"
      "s_lshr_b32 s5, %[bid], 4\n\t"
      "s_lshl4_add_u32 s36, s4, s5\n\t"
      "s_lshl_b32 s6, s36, 19\n\t"
      "v_lshrrev_b32 v11, 4, %[tid]\n\t"
      "v_xor_b32 v1, v11, %[tid]\n\t"
      "v_and_b32 v1, 15, v1\n\t"
      "v_lshlrev_b32 v1, 4, v1\n\t"
      "v_lshl_or_b32 v1, v11, 8, v1\n\t"
      "v_xor_b32 v2, 64, v1\n\t"
      "v_xor_b32 v3, 0x80, v1\n\t"
      "v_xor_b32 v4, 0xc0, v1\n\t"
      "s_mov_b32 s26, 0x80000\n\t"
      "s_mov_b32 s27, 0x20000\n\t"
      "s_mov_b32 s37, 0\n\t"
      "s_mov_b32 m0, 0\n\t"
      "s_waitcnt lgkmcnt(0)\n\t"
      "s_add_u32 s24, s8, s6\n\t"
      "s_addc_u32 s25, s9, 0\n\t"
      "s_and_b32 s25, s25, 0xffff\n\t"
      KGRP0 KGRP0 KGRP0 KGRP0
      "s_lshl_b32 s7, s36, 13\n\t"
      "s_add_u32 s32, s12, s7\n\t"
      "s_addc_u32 s33, s13, 0\n\t"
      "s_and_b32 s33, s33, 0xffff\n\t"
      "s_movk_i32 s34, 0x2000\n\t"
      "s_mov_b32 s35, s27\n\t"
      "s_mov_b32 m0, %c[lq]\n\t"
      "s_movk_i32 s39, 0x1000\n\t"
      LD4("v1", "v2", "v3", "v4", "s[32:35]", "0")
      "s_add_u32 m0, m0, 0x1000\n\t"
      "s_lshl_b32 s5, s36, 11\n\t"
      LD4("v1", "v2", "v3", "v4", "s[32:35]", "s39")
      "s_add_u32 s32, s16, s5\n\t"
      "s_addc_u32 s33, s17, 0\n\t"
      "s_and_b32 s33, s33, 0xffff\n\t"
      "s_movk_i32 s34, 0x800\n\t"
      "s_add_u32 m0, m0, 0x1000\n\t"
      "v_lshrrev_b32 v11, 3, %[tid]\n\t"
      "buffer_load_dwordx4 v1, s[32:35], 0 offen nt lds\n\t"
      "buffer_load_dwordx4 v2, s[32:35], 0 offen offset:1024 nt lds\n\t"
      "v_and_b32 v5, 6, v11\n\t"
      "v_xor_b32 v5, v5, %[tid]\n\t"
      "v_and_b32 v5, 7, v5\n\t"
      "v_lshlrev_b32 v5, 4, v5\n\t"
      "v_lshl_or_b32 v5, v11, 8, v5\n\t"
      "s_and_b32 s4, %[bid], 8\n\t"
      "s_lshl_b32 s4, s4, 4\n\t"
      "v_or_b32 v5, s4, v5\n\t"
      "v_add_u32 v6, 0x400, v5\n\t"
      "v_add_u32 v7, 0x800, v5\n\t"
      "v_add_u32 v8, 0xc00, v5\n\t"
      "s_add_u32 s32, s18, s5\n\t"
      "s_addc_u32 s33, s19, 0\n\t"
      "s_and_b32 s33, s33, 0xffff\n\t"
      "s_add_u32 m0, m0, 0x800\n\t"
      "v_lshlrev_b32 v10, 2, %[tid]\n\t"
      "buffer_load_dwordx4 v5, s[32:35], 0 offen nt lds\n\t"
      "s_lshr_b32 s4, s36, 3\n\t"
      "s_lshl_b32 s4, s4, 7\n\t"
      "s_add_u32 s32, s20, s4\n\t"
      "s_addc_u32 s33, s21, 0\n\t"
      "s_and_b32 s33, s33, 0xffff\n\t"
      "s_movk_i32 s34, 0x80\n\t"
      "s_add_u32 m0, m0, 0x400\n\t"
      "s_add_u32 s28, s10, s6\n\t"
      "buffer_load_dword v10, s[32:35], 0 offen nt lds\n\t"
      "s_addc_u32 s29, s11, 0\n\t"
      "s_and_b32 s29, s29, 0xffff\n\t"
      "s_mov_b64 s[30:31], s[26:27]\n\t"
      "s_mov_b32 s38, 0\n\t"
      "v_add_u32 v10, %c[fl], v10\n\t"
      "v_mov_b32 v9, %c[n0]\n\t"
      "v_mov_b32 v11, %c[fc]\n\t"
      "s_add_u32 s22, s22, s7\n\t"
      "s_addc_u32 s23, s23, 0\n\t"
      "v_mov_b32 v12, s22\n\t"
      "v_mov_b32 v13, s23\n\t"
      "ds_write_b32 v10, v9\n\t"
      "ds_write_b64 v11, v[12:13] offset:%c[lo]\n\t"
      "s_mov_b32 s40, %c[sl1]\n\t"
      "s_mov_b32 s41, %c[need0]\n\t"
      "s_mov_b32 m0, s40\n\t"
      "s_waitcnt lgkmcnt(0)\n\t"
      "s_barrier\n\t"
      KGRP KGRP KGRP KGRP
      "s_add_u32 s40, s40, %c[slot]\n\t"
      "s_mov_b32 m0, s40\n\t"
      ".Lstep%=:\n\t"
      "ds_read_b128 v[12:15], v11\n\t"
      KGRP KGRP KGRP KGRP VGRP VGRP
      "s_add_u32 s40, s40, %c[slot]\n\t"
      "s_cmp_eq_u32 s40, %c[ring]\n\t"
      "s_cselect_b32 s40, 0, s40\n\t"
      ".Lfree%=:\n\t"
      "s_waitcnt lgkmcnt(0)\n\t"
      "v_min_i32 v12, v12, v13\n\t"
      "v_min3_i32 v12, v12, v14, v15\n\t"
      "s_mov_b32 m0, s40\n\t"
      "v_readfirstlane_b32 s4, v12\n\t"
      "s_cmp_ge_i32 s4, s41\n\t"
      "s_cbranch_scc1 .Lgo%=\n\t"
      "ds_read_b128 v[12:15], v11\n\t"
      "s_branch .Lfree%=\n\t"
      ".Lgo%=:\n\t"
      "s_add_i32 s41, s41, 1\n\t"
      "s_cmp_lt_u32 s37, 0x80000\n\t"
      "s_cbranch_scc1 .Lstep%=\n\t"
      "s_add_u32 m0, m0, 0x4000\n\t"
      VGRP VGRP
      "s_mov_b32 m0, s40\n\t"
      VGRP VGRP
      DRAIN(28) DRAIN(24) DRAIN(20) DRAIN(16) DRAIN(12) DRAIN(8)
      "s_waitcnt vmcnt(0)\n\t"
      "s_barrier\n\t"
      "s_endpgm\n\t"
      ".Lcompute%=:"
      :: [ka] "s"(__builtin_amdgcn_kernarg_segment_ptr()), [bid] "s"(blockIdx.x), [tid] "v"(threadIdx.x),
         [wn] "i"(W - 4), [lq] "i"(L_Q), [fl] "i"(F_LAND), [fc] "i"(F_CONS), [n0] "i"(7 - LAG), [sl1] "i"(SLOT), [slot] "i"(SLOT), [ring] "i"(RING),
         [need0] "i"(AH + 1 - (NSLOT - 1)), [lo] "i"(L_OUT - F_CONS)
      : "memory", "scc", "vcc", "s4", "s5", "s6", "s7", "s8", "s9", "s10", "s11", "s12", "s13", "s14", "s15", "s16", "s17", "s18", "s19", "s20", "s21", "s22", "s23",
        "s24", "s25", "s26", "s27", "s28", "s29", "s30", "s31", "s32", "s33", "s34", "s35", "s36", "s37", "s38", "s39", "s40", "s41",
        "v1", "v2", "v3", "v4", "v5", "v6", "v7", "v8", "v9", "v10", "v11", "v12", "v13", "v14", "v15", "v16");
  asm volatile("" :: "v"((LDSP(char))lds));
  const int lane = threadIdx.x & 63, w = __builtin_amdgcn_readfirstlane(threadIdx.x >> 6);
  const int dh = (blockIdx.x >> 3) & 1;
  const int r = lane & 15, g = lane >> 4;
  const int cw = w - 1, h = cw >> 1, u = cw & 1;
  const unsigned fcons = F_CONS + lane * 16 + cw * 4;
  *(volatile LDSP(int))fcons = 0;
  unsigned ka[4], va[4];
#pragma unroll
  for (int s = 0; s < 4; s++) ka[s] = r * 256 + (((4 * s + g) ^ r) * 16);
  const int vrow = 4 * g + (r >> 2);
#pragma unroll
  for (int c = 0; c < 4; c++) va[c] = vrow * 128 + ((((2 * c) ^ (4 * u) ^ ((r >> 1) & 1)) ^ (vrow & 6)) * 16) + (r & 1) * 8;
  const u32x4 ones = {0x3f803f80u, 0x3f803f80u, 0x3f803f80u, 0x3f803f80u};
  f32x4 o[4], lacc = {0.f, 0.f, 0.f, 0.f};
#pragma unroll
  for (int c = 0; c < 4; c++) o[c] = f32x4{0.f, 0.f, 0.f, 0.f};
  float m = -INFINITY;
  int land = -100;
  const int ooff = (16 * h + r) * 256 + dh * 128 + u * 64 + (g & 1) * 32 + (g >> 1) * 16;
  asm volatile("s_waitcnt lgkmcnt(0)" ::: "memory");
  __builtin_amdgcn_s_barrier();
  asm volatile("" ::: "memory");
  const u32x2 op = *(LDSP(u32x2))L_OUT;
  __amdgpu_buffer_rsrc_t ro = __builtin_amdgcn_make_buffer_rsrc(
      (char*)((unsigned long long)(unsigned)__builtin_amdgcn_readfirstlane(op[1]) << 32 | (unsigned)__builtin_amdgcn_readfirstlane(op[0])), 0, 8192, 0x00020000);
  land_wait(7, land);
  u32x4 qf[4];
#pragma unroll
  for (int s = 0; s < 4; s++) qf[s] = *(LDSP(u32x4))(ka[s] + L_Q + h * 4096);
  if (u == 0) {
    const u32x2 mk = *(LDSP(u32x2))(L_MK + (r & 7) * 16 + (g & 1) * 8);
    f32x4 s4;
#pragma unroll
    for (int i = 0; i < 4; i++) {
      const float mv = __uint_as_float((i & 1) ? (mk[i >> 1] & 0xffff0000u) : (mk[i >> 1] << 16));
      s4[i] = g < 2 ? (1.f - mv) * MSKRAW : -INFINITY;
    }
#pragma unroll
    for (int s = 0; s < 4; s++) s4 = mfma16(*(LDSP(u32x4))(L_KT + (r & 7) * 256 + (((4 * s + g) ^ (r & 7)) * 16)), qf[s], s4);
    m = xg_max(mx2(mx2(s4[0], s4[1]), mx2(s4[2], s4[3]))) * SC2;
    const u32x4 pt = {pk2(__builtin_amdgcn_exp2f(s4[0] * SC2 - m), __builtin_amdgcn_exp2f(s4[1] * SC2 - m)),
                      pk2(__builtin_amdgcn_exp2f(s4[2] * SC2 - m), __builtin_amdgcn_exp2f(s4[3] * SC2 - m)), 0u, 0u};
    lacc = mfma16(ones, pt, lacc);
#pragma unroll
    for (int c = 0; c < 4; c++) {
      const u32x2 x = __builtin_bit_cast(u32x2, __builtin_amdgcn_ds_read_tr16_b64_v4i16((LDSP(s16x4))(va[c] - (g >> 1) * 1024 + L_VT)));
      o[c] = mfma16(u32x4{x[0], x[1], x[0], x[1]}, pt, o[c]);
    }
  }
#pragma unroll
  for (int s = 0; s < 4; s++) { ka[s] += u * 8192; va[s] += u * 4096; }
  auto qk = [&](unsigned sb, u32x4& pf, float& alpha) {
    unsigned ad[4];
#pragma unroll
    for (int k = 0; k < 4; k++) { ad[k] = ka[k] + sb; asm volatile("" : "+v"(ad[k])); }
    u32x4 kf[2][4];
#pragma unroll
    for (int k = 0; k < 4; k++)
#pragma unroll
      for (int t = 0; t < 2; t++) kf[t][k] = *(LDSP(u32x4))(ad[k] + t * 4096);
    __builtin_amdgcn_sched_barrier(0);
    f32x4 s0 = {0.f, 0.f, 0.f, 0.f}, s1 = s0;
#pragma unroll
    for (int k = 0; k < 4; k++) { s0 = mfma16(kf[0][k], qf[k], s0); s1 = mfma16(kf[1][k], qf[k], s1); }
    const float mx = mx2(mx2(mx2(mx2(s0[0], s0[1]), s0[2]), s0[3]), mx2(mx2(mx2(s1[0], s1[1]), s1[2]), s1[3]));
    const float mn = mx2(m, xg_max(mx) * SC2);
    alpha = __builtin_amdgcn_exp2f(m - mn);
    m = mn;
    pf = u32x4{pk2(__builtin_amdgcn_exp2f(s0[0] * SC2 - mn), __builtin_amdgcn_exp2f(s0[1] * SC2 - mn)),
               pk2(__builtin_amdgcn_exp2f(s0[2] * SC2 - mn), __builtin_amdgcn_exp2f(s0[3] * SC2 - mn)),
               pk2(__builtin_amdgcn_exp2f(s1[0] * SC2 - mn), __builtin_amdgcn_exp2f(s1[1] * SC2 - mn)),
               pk2(__builtin_amdgcn_exp2f(s1[2] * SC2 - mn), __builtin_amdgcn_exp2f(s1[3] * SC2 - mn))};
    lacc[0] *= alpha;
    lacc = mfma16(ones, pf, lacc);
  };
  auto rescale = [&](float alpha) {
    if (__any(alpha != 1.f)) {
#pragma unroll
      for (int c = 0; c < 4; c++) o[c] *= alpha;
    }
  };
  auto pv = [&](unsigned sb, u32x4 p) {
    u32x4 vf[4];
#pragma unroll
    for (int c = 3; c >= 0; c--) { unsigned ad = va[c] + sb; asm volatile("" : "+v"(ad)); vf[c] = tr_read2(ad + 16384); }
    __builtin_amdgcn_sched_barrier(0);
#pragma unroll
    for (int c = 3; c >= 0; c--) o[c] = mfma16(vf[c], p, o[c]);
  };
  unsigned xw = L_Q + lane * 16 + cw * 3072, xr = L_Q + lane * 16 + (cw ^ 1) * 3072, sw = L_ML + lane * 8 + cw * 512, sr = L_ML + lane * 8 + (cw ^ 1) * 512;
  unsigned oo = ooff, vb0 = va[0] ^ 4096, vb1 = va[1] ^ 4096;
  asm volatile("" : "+v"(xw), "+v"(xr), "+v"(sw), "+v"(sr), "+v"(oo), "+v"(vb0), "+v"(vb1), "+v"(va[0]), "+v"(va[1]));
  u32x4 pq[AH + 1];
  float aq[AH + 1];
  qk(0, pq[0], aq[0]);
  if (AH == 2) { land_wait(kpos(1) + 2 + 2 * u, land); qk(SLOT, pq[1], aq[1]); }
  unsigned sb = AH * SLOT;
  int need = kpos(AH) + 2 + 2 * u;
#pragma unroll 1
  for (int i = AH; i < NT; i++) {
    land_wait(need, land);
    qk(sb, pq[AH], aq[AH]);
    rescale(aq[0]);
    land_wait(need + 3 - u, land);
    pv(sb, pq[0]);
    asm volatile("" ::: "memory");
    *(volatile LDSP(int))fcons = i + 1;
#pragma unroll
    for (int a = 0; a < AH; a++) { pq[a] = pq[a + 1]; aq[a] = aq[a + 1]; }
    need += 6; sb += SLOT; if (sb == RING) sb = 0;
  }
  *(LDSP(u32x4))(xw + 2048) = pq[AH - 1];
  *(LDSP(f32x2))(sw) = f32x2{m, lacc[0]};
  if (AH == 2) {
    rescale(aq[0]);
    land_wait(NGRP - 3 + u, land);
    pv(L_TAIL, pq[0]);
  }
  rescale(aq[AH - 1]);
  asm volatile("s_nop 4" : "+v"(o[2]), "+v"(o[3]));
  *(LDSP(f32x4))(xw + 1024) = o[3];
  *(LDSP(f32x4))(xw) = o[2];
  asm volatile("s_waitcnt lgkmcnt(0)" ::: "memory");
  __builtin_amdgcn_s_barrier();
  asm volatile("" ::: "memory");
  const u32x4 v00 = tr_read2(va[0] + L_TAIL), v01 = tr_read2(va[1] + L_TAIL);
  const f32x2 ml = *(LDSP(f32x2))(sr);
  const u32x4 po = *(LDSP(u32x4))(xr + 2048);
  const u32x4 v10 = tr_read2(vb0 + L_TAIL), v11 = tr_read2(vb1 + L_TAIL);
  f32x4 x0 = *(LDSP(f32x4))(xr), x1 = *(LDSP(f32x4))(xr + 1024);
  __builtin_amdgcn_sched_barrier(0);
  o[0] = mfma16(v00, pq[AH - 1], o[0]); o[1] = mfma16(v01, pq[AH - 1], o[1]);
  const float M = mx2(m, ml[0]);
  float a = __builtin_amdgcn_exp2f(m - M), b = __builtin_amdgcn_exp2f(ml[0] - M);
  const float inv = __builtin_amdgcn_rcpf(lacc[0] * a + ml[1] * b);
  a *= inv; b *= inv;
  x0 = mfma16(v10, po, x0); x1 = mfma16(v11, po, x1);
  f32x4 y0 = o[0] * a, y1 = o[1] * a;
  asm volatile("" : "+v"(y0), "+v"(y1));
  y0 += x0 * b; y1 += x1 * b;
  auto e0 = __builtin_amdgcn_permlane16_swap(pk2(y0[0], y0[1]), pk2(y1[0], y1[1]), false, false);
  auto e1 = __builtin_amdgcn_permlane16_swap(pk2(y0[2], y0[3]), pk2(y1[2], y1[3]), false, false);
  __builtin_amdgcn_raw_buffer_store_b128(u32x4{e0[0], e1[0], e0[1], e1[1]}, ro, oo, 0, 16);
  asm volatile(".Lcode1:");
}

extern "C" void kernel_launch(void* const* d_in, const int* in_sizes, int n_in, void* d_out, int out_size, void* d_ws, size_t ws_size, hipStream_t stream) {
  tree_attn<<<256, 320, 0, stream>>>((const char*)d_in[1], (const char*)d_in[2], (const char*)d_in[0], (char*)d_ws, (const char*)d_in[3],
                                     (const char*)d_in[4], (const char*)d_in[5], (char*)d_out);
}
```
